```python
import torch

def _softmax(x, axis=-1):
    return torch.softmax(x, dim=axis)
def _gelu(x):
    return torch.nn.functional.gelu(x, approximate="tanh")
def _silu(x):
    return torch.nn.functional.silu(x)
def _sigmoid(x):
    return torch.sigmoid(x)
def _layernorm(x, axis=-1, eps=1e-5):
    m = x.mean(dim=axis, keepdim=True)
    v = x.var(dim=axis, keepdim=True, unbiased=False)
    return (x - m) / torch.sqrt(v + eps)
def _scatter_add(tgt, idx, src, axis=0):
    return tgt.clone().index_add_(axis, idx, src)
def _segment_sum(src, seg, n):
    out = torch.zeros((n,) + src.shape[1:], dtype=src.dtype)
    return out.index_add_(0, seg, src)
def _pad(x, pw):
    flat = tuple(v for pair in reversed(pw) for v in pair)
    return torch.nn.functional.pad(x, flat)

kernel_name = 'standalone__w4a16_gemm_dequant__prompt'

def setup_inputs():

    _w = (lambda a: (lambda t: t.to(torch.bfloat16) if t.is_floating_point() else t)(torch.as_tensor(a)))
    B, S, D, F = 1, 2048, 576, 4608
    G = 32 if 32 > 0 else D
    D = (D // G) * G
    ng = D // G
    qw = torch.randint(0, 16, (D, F)).to(torch.int32)
    return {
        "x": _w((lambda *s: torch.randn(*s, dtype=torch.bfloat16) * 0.05)(B, S, D)),
        "qw": _w(qw),
        "scales": _w(torch.abs((lambda *s: torch.randn(*s, dtype=torch.bfloat16) * 0.05)(ng, F)) + 0.01),
        "zeros": _w(torch.full((ng, F), 8.0, dtype=float)),
    }

def reference(x, qw, scales, zeros):
    B, S, D = x.shape
    ng, F = scales.shape
    G = D // ng
    s = (lambda x, n, axis: torch.repeat_interleave(x, n, dim=axis))(scales, G, axis=0)
    z = (lambda x, n, axis: torch.repeat_interleave(x, n, dim=axis))(zeros, G, axis=0)
    w = ((lambda x, dt: x.to(dt))(qw, x.dtype) - z) * s
    return x @ w
```

```cpp
#include <hip/hip_runtime.h>
#include <hip/hip_bf16.h>
#include <stdint.h>

typedef float    f32x4  __attribute__((ext_vector_type(4)));
typedef float    f32x2  __attribute__((ext_vector_type(2)));
typedef __bf16   bf16x8 __attribute__((ext_vector_type(8)));
typedef __bf16   bf16x2 __attribute__((ext_vector_type(2)));
typedef unsigned u32x4  __attribute__((ext_vector_type(4)));
typedef unsigned u32x2  __attribute__((ext_vector_type(2)));
typedef int      i32x4  __attribute__((ext_vector_type(4)));

#define DI __device__ __forceinline__
#define LDS_AS __attribute__((address_space(3)))

constexpr int M_ = 2048, K_ = 576, N_ = 4608;
constexpr int NST = 9;
constexpr int A_STAGE = 32768;
constexpr int B_STAGE = 18432;
constexpr int A_OFF = 0, B_OFF = 3 * A_STAGE, SZ_OFF = B_OFF + 2 * B_STAGE, SZX_OFF = SZ_OFF + 9216;
constexpr int LDS_BYTES = SZ_OFF + 12288;

DI unsigned rfl(unsigned x) { return __builtin_amdgcn_readfirstlane(x); }
DI unsigned cvtpk(float lo, float hi) {
  f32x2 v = {lo, hi};
  return __builtin_bit_cast(unsigned, __builtin_convertvector(v, bf16x2));
}
DI float bflo(unsigned u) { return __builtin_bit_cast(float, u << 16); }
DI float bfhi(unsigned u) { return __builtin_bit_cast(float, u & 0xffff0000u); }
DI float fma_s(float a, float b, float c) { float r; asm("v_fma_f32 %0, %1, %2, %3" : "=v"(r) : "v"(a), "v"(b), "v"(c)); return r; }

template <int N> DI void wait_vm() { asm volatile("s_waitcnt vmcnt(%0)" ::"n"(N) : "memory"); }
DI void wait_lgkm0() { asm volatile("s_waitcnt lgkmcnt(0)" ::: "memory"); }
DI void barrier() { __builtin_amdgcn_s_barrier(); asm volatile("" ::: "memory"); }
#define GAP() __builtin_amdgcn_sched_barrier(0)

DI void dma16(unsigned m0v, unsigned voff, __amdgpu_buffer_rsrc_t srd, unsigned soff) {
  asm volatile("s_mov_b32 m0, %0\n\ts_nop 0\n\tbuffer_load_dwordx4 %1, %2, %3 offen lds" ::"s"(m0v), "v"(voff), "s"(srd), "s"(soff) : "m0", "memory");
}
DI void dma16g(unsigned m0v, const void* p) {
  asm volatile("s_mov_b32 m0, %0\n\ts_nop 0\n\tglobal_load_lds_dwordx4 %1, off" ::"s"(m0v), "v"(p) : "m0", "memory");
}
DI void ld4(i32x4& d, unsigned voff, __amdgpu_buffer_rsrc_t srd, unsigned soff) {
  asm volatile("buffer_load_dwordx4 %0, %1, %2, %3 offen" : "=v"(d) : "v"(voff), "s"(srd), "s"(soff) : "memory");
}
DI void ld1(int& d, unsigned voff, __amdgpu_buffer_rsrc_t srd, unsigned soff) {
  asm volatile("buffer_load_dword %0, %1, %2, %3 offen" : "=v"(d) : "v"(voff), "s"(srd), "s"(soff) : "memory");
}

struct Raw { i32x4 q[8]; int e[4]; };

template <int ST> DI void load_raw(Raw& r, unsigned voffB, unsigned voffX, __amdgpu_buffer_rsrc_t srdQ) {
#pragma unroll
  for (int j = 0; j < 8; ++j) ld4(r.q[j], voffB, srdQ, (unsigned)((64 * ST + j) * N_ * 4));
#pragma unroll
  for (int j = 0; j < 4; ++j) ld1(r.e[j], voffX, srdQ, (unsigned)((64 * ST + j) * N_ * 4));
}
DI void pin_raw(Raw& r) {
  asm volatile("" : "+v"(r.q[0]), "+v"(r.q[1]), "+v"(r.q[2]), "+v"(r.q[3]), "+v"(r.q[4]), "+v"(r.q[5]), "+v"(r.q[6]), "+v"(r.q[7]),
               "+v"(r.e[0]), "+v"(r.e[1]), "+v"(r.e[2]), "+v"(r.e[3]));
}
template <int ST> DI void load_a(unsigned ldsb, unsigned w, unsigned vA0, unsigned vA1, __amdgpu_buffer_rsrc_t srdX) {
#pragma unroll
  for (int pc = 0; pc < 8; ++pc)
    dma16(ldsb + A_OFF + (ST % 3) * A_STAGE + pc * 1024 + w * 8192, (pc & 1) ? vA1 : vA0, srdX, (unsigned)(pc * 8 * K_ * 2 + ST * 128));
}

struct SZ { u32x2 s, z; unsigned sx, zx; };
template <int ST> DI void read_sz(SZ& o, LDS_AS char* lds, unsigned szm, unsigned szx) {
  o.s = *(LDS_AS u32x2*)(lds + szm + ST * 1024);
  o.z = *(LDS_AS u32x2*)(lds + szm + ST * 1024 + 256);
  o.sx = *(LDS_AS unsigned short*)(lds + szx + ST * 128);
  o.zx = *(LDS_AS unsigned short*)(lds + szx + ST * 128 + 32);
}
template <int ST> DI void dequant(const Raw& r, const SZ& p, LDS_AS char* lds, unsigned wB, unsigned wX) {
  float s[4] = {bflo(p.s[0]), bfhi(p.s[0]), bflo(p.s[1]), bfhi(p.s[1])};
  float z[4] = {bflo(p.z[0]), bfhi(p.z[0]), bflo(p.z[1]), bfhi(p.z[1])};
#pragma unroll
  for (int i = 0; i < 4; ++i) {
    const float nzs = -z[i] * s[i];
    u32x4 f;
#pragma unroll
    for (int j = 0; j < 4; ++j)
      f[j] = cvtpk(fma_s((float)r.q[2 * j][i], s[i], nzs), fma_s((float)r.q[2 * j + 1][i], s[i], nzs));
    *(LDS_AS u32x4*)(lds + wB + (ST & 1) * B_STAGE + i * 1024) = f;
  }
  const float sx = bflo(p.sx), nzx = -bflo(p.zx) * sx;
  u32x2 g;
  g[0] = cvtpk(fma_s((float)r.e[0], sx, nzx), fma_s((float)r.e[1], sx, nzx));
  g[1] = cvtpk(fma_s((float)r.e[2], sx, nzx), fma_s((float)r.e[3], sx, nzx));
  *(LDS_AS u32x2*)(lds + wX + (ST & 1) * B_STAGE) = g;
}

struct Cons {
  f32x4 acc[4][9];
  bf16x8 a[2][4];
  bf16x8 b[4];
  unsigned rA[2], rB[2];
};
template <int KS, int I> DI void rd_a(Cons& c, LDS_AS char* lds) {
  c.a[KS & 1][I] = *(LDS_AS bf16x8*)(lds + c.rA[KS & 1] + ((KS >> 1) % 3) * A_STAGE + I * 2048);
}
template <int S> DI void rd_b(Cons& c, LDS_AS char* lds) {
  constexpr int KS = S / 9, T = S % 9;
  c.b[S & 3] = *(LDS_AS bf16x8*)(lds + c.rB[(T >> 2) & 1] + ((KS >> 1) & 1) * B_STAGE + (KS & 1) * 9216 + T * 1024);
}
template <int S> DI void slot(Cons& c, LDS_AS char* lds) {
  constexpr int KS = S / 9, T = S % 9, NS = 18 * 9;
  if constexpr (T == 6 && (KS & 1) && KS + 1 < 18) {
    if constexpr (KS == 1) wait_vm<0>();
    wait_lgkm0();
    barrier();
    GAP();
  }
  if constexpr (KS + 1 < 18) {
    if constexpr (T == 6) { rd_a<KS + 1, 0>(c, lds); rd_a<KS + 1, 1>(c, lds); }
    if constexpr (T == 7) { rd_a<KS + 1, 2>(c, lds); rd_a<KS + 1, 3>(c, lds); }
  }
  if constexpr (S + 3 < NS) rd_b<S + 3>(c, lds);
#pragma unroll
  for (int i = 0; i < 4; ++i) {
    if constexpr (T < 8) c.acc[i][T] = __builtin_amdgcn_mfma_f32_16x16x32_bf16(c.a[KS & 1][i], c.b[S & 3], c.acc[i][T], 0, 0, 0);
    else                 c.acc[i][T] = __builtin_amdgcn_mfma_f32_16x16x32_bf16(c.b[S & 3], c.a[KS & 1][i], c.acc[i][T], 0, 0, 0);
  }
  GAP();
  if constexpr (S + 1 < NS) slot<S + 1>(c, lds);
}

__global__ __launch_bounds__(512, 2) void w4a16_kernel(const uint16_t* __restrict__ x, const int* __restrict__ qw,
                                                      const uint16_t* __restrict__ sc, const uint16_t* __restrict__ zr,
                                                      uint16_t* __restrict__ out) {
  __shared__ __attribute__((aligned(1024))) char smem[LDS_BYTES];
  LDS_AS char* lds = (LDS_AS char*)smem;
  const unsigned ldsb = rfl((unsigned)(uintptr_t)lds);
  const unsigned lane = threadIdx.x & 63, wave = rfl(threadIdx.x >> 6), w = wave & 3;
  const unsigned bid = blockIdx.x, sb = bid & 7, jn = (bid >> 3) & 3, mb = bid >> 5;
  const unsigned nmain = 576 * sb + 128 * jn, next = 576 * sb + 512 + 16 * jn, m0 = 256 * mb;

  const __amdgpu_buffer_rsrc_t srdX = __builtin_amdgcn_make_buffer_rsrc((void*)x, 0, M_ * K_ * 2, 0x00020000);
  const unsigned vA0 = (m0 + 64 * w + (lane >> 3)) * (K_ * 2) + (((lane & 7) ^ (lane >> 4)) << 4), vA1 = vA0 ^ 64;

  if (wave >= 4) {
    const __amdgpu_buffer_rsrc_t srdQ = __builtin_amdgcn_make_buffer_rsrc((void*)qw, 0, K_ * N_ * 4, 0x00020000);
    const unsigned Q = lane & 31, o = 2 * w + (lane >> 5), c = lane & 15, kq = lane >> 4, ks = w >> 1;
    const unsigned voffB = (8 * o * N_ + nmain + 4 * Q) * 4;
    const unsigned voffX = ((16 * w + 4 * kq) * N_ + next + c) * 4;
    const unsigned wB = B_OFF + ks * 9216 + (Q & 1) * 4096 + ((((o & 3) * 16 + (Q >> 1)) ^ (4 * (Q & 1))) << 4);
    const unsigned wX = B_OFF + ks * 9216 + 8192 + (((2 * (w & 1) + (kq >> 1)) * 16 + c) << 4) + (kq & 1) * 8;
    const unsigned szm = SZ_OFF + ks * 512 + Q * 8, szx = SZX_OFF + ks * 64 + c * 2;
    Raw r0, r1, r2;
    SZ p;
    load_raw<0>(r0, voffB, voffX, srdQ);
    load_raw<1>(r1, voffB, voffX, srdQ);
    load_raw<2>(r2, voffB, voffX, srdQ);
    barrier();
    read_sz<0>(p, lds, szm, szx);
    wait_vm<24>(); pin_raw(r0); dequant<0>(r0, p, lds, wB, wX); GAP();
    load_raw<3>(r0, voffB, voffX, srdQ);
    wait_lgkm0(); barrier();
#define PERIOD(P, R, WB, WA)                                                     \
    if constexpr (P + 2 < NST) load_a<P + 2>(ldsb, w, vA0, vA1, srdX);           \
    read_sz<P + 1>(p, lds, szm, szx);                                            \
    if constexpr (WB >= 0) wait_vm<(WB >= 0 ? WB : 0)>();                        \
    pin_raw(R); dequant<P + 1>(R, p, lds, wB, wX); GAP();                        \
    if constexpr (P + 4 < NST) load_raw<(P + 4 < NST ? P + 4 : 0)>(R, voffB, voffX, srdQ); \
    if constexpr (WA >= 0) wait_vm<(WA >= 0 ? WA : 0)>();                        \
    wait_lgkm0(); barrier();
    PERIOD(0, r1, 32, -1)
    PERIOD(1, r2, 40, 32)
    PERIOD(2, r0, -1, 32)
    PERIOD(3, r1, -1, 32)
    PERIOD(4, r2, -1, 32)
    PERIOD(5, r0, -1, 20)
    PERIOD(6, r1, -1, 8)
    PERIOD(7, r2, -1, 0)
#undef PERIOD
  } else {
    __builtin_amdgcn_s_setprio(2);
#pragma unroll
    for (int i = 0; i < 3; ++i) {
      unsigned idx = (w * 3 + i) * 64 + lane;
      idx = idx < 647 ? idx : 647;
      const unsigned e = idx - 576;
      const bool mainp = idx < 576;
      const unsigned R = mainp ? (idx >> 4) : (e >> 1);
      const unsigned off = (R >> 1) * N_ + (mainp ? nmain + 8 * (idx & 15) : next + 8 * (e & 1));
      dma16g(ldsb + SZ_OFF + (w * 3 + i) * 1024, ((R & 1) ? zr : sc) + off);
    }
    load_a<0>(ldsb, w, vA0, vA1, srdX);
    load_a<1>(ldsb, w, vA0, vA1, srdX);
    Cons c;
    const unsigned r = lane & 15, q = lane >> 4;
    c.rA[0] = A_OFF + (64 * w + r) * 128 + ((q ^ (r >> 1)) << 4);
    c.rA[1] = c.rA[0] ^ 64;
    c.rB[0] = B_OFF + lane * 16;
    c.rB[1] = B_OFF + (lane ^ 4) * 16;
#pragma unroll
    for (int i = 0; i < 4; ++i)
#pragma unroll
      for (int t = 0; t < 9; ++t) c.acc[i][t] = f32x4{0.f, 0.f, 0.f, 0.f};
    wait_vm<16>(); barrier();
    wait_vm<8>(); barrier();
    rd_a<0, 0>(c, lds); rd_b<0>(c, lds); rd_a<0, 1>(c, lds); rd_a<0, 2>(c, lds); rd_a<0, 3>(c, lds);
    rd_b<1>(c, lds); rd_b<2>(c, lds);
    GAP();
    slot<0>(c, lds);

    uint16_t* om = out + (size_t)(m0 + 64 * w + 4 * q) * N_ + nmain + 8 * r;
    uint16_t* ox = out + (size_t)(m0 + 64 * w + r) * N_ + next + 4 * q;
#pragma unroll
    for (int i = 0; i < 4; ++i) {
#pragma unroll
      for (int g = 0; g < 4; ++g) {
        u32x4 v;
#pragma unroll
        for (int t = 0; t < 4; ++t) v[t] = cvtpk(c.acc[i][2 * t][g], c.acc[i][2 * t + 1][g]);
        *(u32x4*)(om + (16 * i + g) * N_) = v;
      }
      u32x2 u = {cvtpk(c.acc[i][8][0], c.acc[i][8][1]), cvtpk(c.acc[i][8][2], c.acc[i][8][3])};
      *(u32x2*)(ox + 16 * i * N_) = u;
    }
  }
}

extern "C" void kernel_launch(void* const* d_in, const int* in_sizes, int n_in, void* d_out, int out_size, void* d_ws,
                              size_t ws_size, hipStream_t stream) {
  (void)in_sizes; (void)n_in; (void)out_size; (void)d_ws; (void)ws_size;
  w4a16_kernel<<<256, 512, 0, stream>>>((const uint16_t*)d_in[0], (const int*)d_in[1], (const uint16_t*)d_in[2],
                                        (const uint16_t*)d_in[3], (uint16_t*)d_out);
}
```

```cpp
#include <hip/hip_runtime.h>
#include <hip/hip_bf16.h>
#include <stdint.h>

typedef float    f32x4  __attribute__((ext_vector_type(4)));
typedef float    f32x2  __attribute__((ext_vector_type(2)));
typedef __bf16   bf16x8 __attribute__((ext_vector_type(8)));
typedef __bf16   bf16x2 __attribute__((ext_vector_type(2)));
typedef unsigned u32x4  __attribute__((ext_vector_type(4)));
typedef unsigned u32x2  __attribute__((ext_vector_type(2)));
typedef int      i32x4  __attribute__((ext_vector_type(4)));
typedef short    s16x4  __attribute__((ext_vector_type(4)));
typedef short    s16x8  __attribute__((ext_vector_type(8)));

#define DI __device__ __forceinline__
#define LDS_AS __attribute__((address_space(3)))

constexpr int M_ = 2048, K_ = 576, N_ = 4608;
constexpr int NST = 9;
constexpr int A_STAGE = 32768;
constexpr int B_STAGE = 18432;
constexpr int A_OFF = 0, B_OFF = 3 * A_STAGE, SZ_OFF = B_OFF + 2 * B_STAGE, SZX_OFF = SZ_OFF + 18 * 1024;
constexpr int LDS_BYTES = SZX_OFF + 18 * 128;

DI unsigned rfl(unsigned x) { return __builtin_amdgcn_readfirstlane(x); }
DI unsigned cvtpk(float lo, float hi) {
  f32x2 v = {lo, hi};
  return __builtin_bit_cast(unsigned, __builtin_convertvector(v, bf16x2));
}
DI float bflo(unsigned u) { return __builtin_bit_cast(float, u << 16); }
DI float bfhi(unsigned u) { return __builtin_bit_cast(float, u & 0xffff0000u); }
DI float dq(int q, float s24, float nzs) {
  float r;
  asm("v_fma_mix_f32 %0, %1, %2, %3 op_sel_hi:[1,0,0]" : "=v"(r) : "v"(q), "v"(s24), "v"(nzs));
  return r;
}

template <int N> DI void wait_vm() { asm volatile("s_waitcnt vmcnt(%0)" ::"n"(N) : "memory"); }
DI void wait_lgkm0() { asm volatile("s_waitcnt lgkmcnt(0)" ::: "memory"); }
DI void barrier() { __builtin_amdgcn_s_barrier(); asm volatile("" ::: "memory"); }
#define GAP() __builtin_amdgcn_sched_barrier(0)
DI void opaque(unsigned& v) { asm volatile("" : "+v"(v)); }

#define STAMP(i)
#define STAMP_RT(i)
#define STAMP_END()
#define SP
#define SA

DI void dma16(unsigned m0v, unsigned voff, __amdgpu_buffer_rsrc_t srd, unsigned soff) {
  asm volatile("s_mov_b32 m0, %0\n\ts_nop 0\n\tbuffer_load_dwordx4 %1, %2, %3 offen lds" ::"s"(m0v), "v"(voff), "s"(srd), "s"(soff) : "m0", "memory");
}
DI void ld4(i32x4& d, unsigned voff, __amdgpu_buffer_rsrc_t srd, unsigned soff) {
  asm volatile("buffer_load_dwordx4 %0, %1, %2, %3 offen" : "=v"(d) : "v"(voff), "s"(srd), "s"(soff) : "memory");
}
#define MST "nt"
DI void st4(const void* p, u32x4 v) { asm volatile("global_store_dwordx4 %0, %1, off " MST "\n\ts_nop 1" ::"v"(p), "v"(v) : "memory"); }
#define XST ""
DI void st2(const void* p, u32x2 v) { asm volatile("global_store_dwordx2 %0, %1, off " XST ::"v"(p), "v"(v) : "memory"); }

struct Raw { i32x4 q[8]; i32x4 e; };
struct SZ { f32x4 s, z, sx, zx; };
struct Prod {
  __amdgpu_buffer_rsrc_t srdX, srdQ;
  unsigned vB[3], vX[3], vA0[3], vA1[3];
  unsigned ldsA, wB, wX, szm, szx;
};
template <int ST, int J> DI void ld_q(const Prod& p, Raw& r) {
  if constexpr (ST < NST) ld4(r.q[J], p.vB[ST / 3], p.srdQ, (unsigned)((64 * (ST % 3) + J) * N_ * 4));
}
template <int ST> DI void ld_x(const Prod& p, Raw& r) {
  if constexpr (ST < NST) ld4(r.e, p.vX[ST / 3], p.srdQ, (unsigned)(64 * (ST % 3) * N_ * 4));
}
template <int ST, int PC> DI void ld_a(const Prod& p) {
  if constexpr (ST < NST)
    dma16(p.ldsA + (ST % 3) * A_STAGE + PC * 1024, (PC & 1) ? p.vA1[ST / 3] : p.vA0[ST / 3], p.srdX, (unsigned)(PC * 8 * K_ * 2 + (ST % 3) * 128));
}
template <int ST> DI void ld_raw(const Prod& p, Raw& r) {
  ld_q<ST, 0>(p, r); ld_q<ST, 1>(p, r); ld_q<ST, 2>(p, r); ld_q<ST, 3>(p, r);
  ld_q<ST, 4>(p, r); ld_q<ST, 5>(p, r); ld_q<ST, 6>(p, r); ld_q<ST, 7>(p, r); ld_x<ST>(p, r);
}
template <int ST> DI void ld_astage(const Prod& p) {
  ld_a<ST, 0>(p); ld_a<ST, 1>(p); ld_a<ST, 2>(p); ld_a<ST, 3>(p); ld_a<ST, 4>(p); ld_a<ST, 5>(p); ld_a<ST, 6>(p); ld_a<ST, 7>(p);
}
DI void pin_raw(Raw& r) {
  asm volatile("" : "+v"(r.q[0]), "+v"(r.q[1]), "+v"(r.q[2]), "+v"(r.q[3]), "+v"(r.q[4]), "+v"(r.q[5]), "+v"(r.q[6]), "+v"(r.q[7]), "+v"(r.e));
}
template <int ST> DI void read_sz(SZ& o, const Prod& p, LDS_AS char* lds) {
  if constexpr (ST < NST) {
    o.s  = *(LDS_AS f32x4*)(lds + p.szm + ST * 2048);
    o.z  = *(LDS_AS f32x4*)(lds + p.szm + ST * 2048 + 512);
    o.sx = *(LDS_AS f32x4*)(lds + p.szx + ST * 256);
    o.zx = *(LDS_AS f32x4*)(lds + p.szx + ST * 256 + 64);
  }
}
template <int JP> DI void dq_pair(u32x4 (&f)[4], const Raw& r, const SZ& z) {
#pragma unroll
  for (int i = 0; i < 4; ++i) f[i][JP] = cvtpk(dq(r.q[2 * JP][i], z.s[i], z.z[i]), dq(r.q[2 * JP + 1][i], z.s[i], z.z[i]));
}
template <int D, int SA_, int SB_, int W> DI void period(const Prod& p, Raw& r, SZ& z, LDS_AS char* lds) {
  u32x4 f[4];
  ld_a<SA_, 0>(p); ld_a<SA_, 1>(p);
  wait_vm<W>(); pin_raw(r); GAP();
  dq_pair<0>(f, r, z); GAP();
  ld_q<SB_, 0>(p, r); ld_q<SB_, 1>(p, r); ld_a<SA_, 2>(p); ld_a<SA_, 3>(p); GAP();
  dq_pair<1>(f, r, z); GAP();
  ld_q<SB_, 2>(p, r); ld_q<SB_, 3>(p, r); ld_a<SA_, 4>(p); ld_a<SA_, 5>(p); GAP();
  dq_pair<2>(f, r, z); GAP();
  ld_q<SB_, 4>(p, r); ld_q<SB_, 5>(p, r); ld_a<SA_, 6>(p); ld_a<SA_, 7>(p); GAP();
  dq_pair<3>(f, r, z);
  u32x2 g = {cvtpk(dq(r.e[0], z.sx[0], z.zx[0]), dq(r.e[1], z.sx[1], z.zx[1])), cvtpk(dq(r.e[2], z.sx[2], z.zx[2]), dq(r.e[3], z.sx[3], z.zx[3]))};
#pragma unroll
  for (int i = 0; i < 4; ++i) *(LDS_AS u32x4*)(lds + p.wB + (D & 1) * B_STAGE + i * 1024) = f[i];
  *(LDS_AS u32x2*)(lds + p.wX + (D & 1) * B_STAGE) = g;
  GAP();
  read_sz<D + 1>(z, p, lds); GAP();
  ld_q<SB_, 6>(p, r); ld_q<SB_, 7>(p, r); ld_x<SB_>(p, r); GAP();
}

struct Cons {
  f32x4 acc[4][9];
  bf16x8 a[2][4];
  bf16x8 b[4];
  unsigned rA[2][2], rB[2], rX;
};
template <int KS, int I> DI void rd_a(Cons& c, LDS_AS char* lds) {
  constexpr int SL = (KS >> 1) % 3;
  c.a[KS & 1][I] = *(LDS_AS bf16x8*)(lds + c.rA[SL >> 1][KS & 1] + (SL & 1) * A_STAGE + I * 2048);
}
template <int S> DI void rd_b(Cons& c, LDS_AS char* lds) {
  constexpr int KS = S / 9, T = S % 9, OFF = ((KS >> 1) & 1) * B_STAGE + (KS & 1) * 9216 + T * 1024;
  if constexpr (T < 8) c.b[S & 3] = *(LDS_AS bf16x8*)(lds + c.rB[T >> 2] + OFF);
  else {
    s16x4 lo = __builtin_amdgcn_ds_read_tr16_b64_v4i16((LDS_AS s16x4*)(lds + c.rX + OFF));
    s16x4 hi = __builtin_amdgcn_ds_read_tr16_b64_v4i16((LDS_AS s16x4*)(lds + c.rX + OFF + 512));
    c.b[S & 3] = __builtin_bit_cast(bf16x8, __builtin_shufflevector(lo, hi, 0, 1, 2, 3, 4, 5, 6, 7));
  }
}
template <int S> DI void slot(Cons& c, LDS_AS char* lds SP) {
  constexpr int KS = S / 9, T = S % 9, NS = 18 * 9;
  if constexpr (T == 6 && (KS & 1) && KS + 1 < 18) {
    wait_lgkm0();
    STAMP(4 + KS / 2);
    barrier();
    GAP();
  }
  if constexpr (KS + 1 < 18) {
    if constexpr (T == 6) { rd_a<KS + 1, 0>(c, lds); rd_a<KS + 1, 1>(c, lds); }
    if constexpr (T == 7) { rd_a<KS + 1, 2>(c, lds); rd_a<KS + 1, 3>(c, lds); }
  }
  if constexpr (S + 3 < NS) rd_b<S + 3>(c, lds);
#pragma unroll
  for (int i = 0; i < 4; ++i) {
    if constexpr (T < 8) c.acc[i][T] = __builtin_amdgcn_mfma_f32_16x16x32_bf16(c.a[KS & 1][i], c.b[S & 3], c.acc[i][T], 0, 0, 0);
    else                 c.acc[i][T] = __builtin_amdgcn_mfma_f32_16x16x32_bf16(c.b[S & 3], c.a[KS & 1][i], c.acc[i][T], 0, 0, 0);
  }
  GAP();
  if constexpr (S + 1 < NS) slot<S + 1>(c, lds SA);
}

__global__ __launch_bounds__(512, 2) void w4a16_kernel(const uint16_t* __restrict__ x, const int* __restrict__ qw,
                                                      const uint16_t* __restrict__ sc, const uint16_t* __restrict__ zr,
                                                      uint16_t* __restrict__ out
) {
  STAMP_RT(0); STAMP(1);
  __shared__ __attribute__((aligned(1024))) char smem[LDS_BYTES];
  LDS_AS char* lds = (LDS_AS char*)smem;
  const unsigned ldsb = rfl((unsigned)(uintptr_t)lds);
  const unsigned lane = threadIdx.x & 63, wave = rfl(threadIdx.x >> 6), w = wave & 3;
  const unsigned bid = blockIdx.x, sb = bid & 7, jn = (bid >> 3) & 3, mb = bid >> 5;
  const unsigned nmain = 576 * sb + 128 * jn, next = 576 * sb + 512 + 16 * jn, m0 = 256 * mb;
  const unsigned rot = (mb + jn) % 3;

  if (wave >= 4) {
    Prod p;
    p.srdX = __builtin_amdgcn_make_buffer_rsrc((void*)x, 0, M_ * K_ * 2, 0x00020000);
    p.srdQ = __builtin_amdgcn_make_buffer_rsrc((void*)qw, 0, K_ * N_ * 4, 0x00020000);
    const unsigned Q = lane & 31, o = 2 * w + (lane >> 5), ks = w >> 1, xr = lane >> 2, xc = lane & 3;
    const unsigned vB = (8 * o * N_ + nmain + 4 * Q) * 4;
    const unsigned vX = ((16 * w + xr) * N_ + next + 4 * xc) * 4;
    const unsigned vA = (m0 + 64 * w + (lane >> 3)) * (K_ * 2) + (((lane & 7) ^ (lane >> 4)) << 4);
#pragma unroll
    for (int g = 0; g < 3; ++g) {
      const unsigned k0 = 192 * (g + rot >= 3 ? g + rot - 3 : g + rot);
      p.vB[g] = vB + k0 * (N_ * 4); p.vX[g] = vX + k0 * (N_ * 4);
      p.vA0[g] = vA + k0 * 2; p.vA1[g] = (vA ^ 64) + k0 * 2;
    }
    p.ldsA = ldsb + A_OFF + w * 8192;
    p.wB = B_OFF + ks * 9216 + (Q & 1) * 4096 + ((((o & 3) * 16 + (Q >> 1)) ^ (4 * (Q & 1))) << 4);
    p.wX = B_OFF + ks * 9216 + 8192 + ((xr >> 2) & 1) * 512 + (2 * (w & 1) + (xr >> 3)) * 128 + (xr & 3) * 32 + xc * 8;
    p.szm = SZ_OFF + ks * 1024 + Q * 16; p.szx = SZX_OFF + ks * 128 + xc * 16;
    Raw r0, r1;
    SZ z;
    ld_raw<0>(p, r0); ld_astage<0>(p); ld_raw<1>(p, r1); ld_astage<1>(p);
    opaque(p.wB); opaque(p.wX); opaque(p.szm); opaque(p.szx);
    STAMP(2);
    barrier();
    read_sz<0>(z, p, lds);
    period<0, NST, 2, 25>(p, r0, z, lds);
    wait_vm<26>(); wait_lgkm0(); STAMP(3); barrier();
#define PERIOD(P, R, W0, W1) period<P + 1, P + 2, P + 3, W0>(p, R, z, lds); wait_vm<W1>(); wait_lgkm0(); STAMP(4 + P); barrier();
    PERIOD(0, r1, 19, 26)
    PERIOD(1, r0, 19, 20)
    PERIOD(2, r1, 19, 20)
    PERIOD(3, r0, 19, 20)
    PERIOD(4, r1, 19, 20)
    PERIOD(5, r0, 19, 20)
    PERIOD(6, r1, 19, 11)
    PERIOD(7, r0, 8, 0)
#undef PERIOD
  } else {
    __builtin_amdgcn_s_setprio(2);
    u32x2 sv[3], zv[3];
    unsigned wa[3], wz[3];
#pragma unroll
    for (int i = 0; i < 3; ++i) {
      unsigned idx = (w * 3 + i) * 64 + lane;
      idx = idx < 647 ? idx : 647;
      const unsigned e = idx - 576;
      const bool mainp = idx < 576;
      const unsigned gl = mainp ? (idx >> 5) : (e >> 2);
      unsigned gg = gl + 6 * rot;
      gg = gg >= 18 ? gg - 18 : gg;
      const unsigned off = gg * N_ + (mainp ? nmain + 4 * (idx & 31) : next + 4 * (e & 3));
      sv[i] = *(const u32x2*)(sc + off);
      zv[i] = *(const u32x2*)(zr + off);
      wa[i] = mainp ? SZ_OFF + gl * 1024 + (idx & 31) * 16 : SZX_OFF + gl * 128 + (e & 3) * 16;
      wz[i] = wa[i] + (mainp ? 512 : 64);
    }
    Cons c;
    const unsigned r = lane & 15, q = lane >> 4;
    c.rA[0][0] = A_OFF + (64 * w + r) * 128 + ((q ^ (r >> 1)) << 4);
    c.rA[0][1] = c.rA[0][0] ^ 64;
    c.rA[1][0] = c.rA[0][0] + 2 * A_STAGE;
    c.rA[1][1] = c.rA[0][1] + 2 * A_STAGE;
    c.rB[0] = B_OFF + lane * 16;
    c.rB[1] = B_OFF + 4096 + (lane ^ 4) * 16 - 4096;
    c.rX = B_OFF + lane * 8;
    opaque(c.rA[0][0]); opaque(c.rA[0][1]); opaque(c.rA[1][0]); opaque(c.rA[1][1]); opaque(c.rB[0]); opaque(c.rB[1]); opaque(c.rX);
#pragma unroll
    for (int i = 0; i < 4; ++i)
#pragma unroll
      for (int t = 0; t < 9; ++t) c.acc[i][t] = f32x4{0.f, 0.f, 0.f, 0.f};
    GAP();
#pragma unroll
    for (int i = 0; i < 3; ++i) {
      const f32x4 s = {bflo(sv[i][0]), bfhi(sv[i][0]), bflo(sv[i][1]), bfhi(sv[i][1])};
      const f32x4 zz = {bflo(zv[i][0]), bfhi(zv[i][0]), bflo(zv[i][1]), bfhi(zv[i][1])};
      *(LDS_AS f32x4*)(lds + wa[i]) = s * 16777216.f;
      *(LDS_AS f32x4*)(lds + wz[i]) = -zz * s;
    }
    wait_lgkm0(); STAMP(2); barrier();
    barrier();
    STAMP(3);
    rd_a<0, 0>(c, lds); rd_b<0>(c, lds); rd_a<0, 1>(c, lds); rd_a<0, 2>(c, lds); rd_a<0, 3>(c, lds);
    rd_b<1>(c, lds); rd_b<2>(c, lds);
    GAP();
    slot<0>(c, lds SA);
    STAMP(12);

    uint16_t* om = out + (size_t)(m0 + 64 * w + 4 * q) * N_ + nmain + 8 * r;
    uint16_t* ox = out + (size_t)(m0 + 64 * w + r) * N_ + next + 4 * q;
#pragma unroll
    for (int i = 0; i < 4; ++i) {
#pragma unroll
      for (int g = 0; g < 4; ++g) {
        u32x4 v;
#pragma unroll
        for (int t = 0; t < 4; ++t) v[t] = cvtpk(c.acc[i][2 * t][g], c.acc[i][2 * t + 1][g]);
        st4(om + (16 * i + g) * N_, v);
      }
      u32x2 u = {cvtpk(c.acc[i][8][0], c.acc[i][8][1]), cvtpk(c.acc[i][8][2], c.acc[i][8][3])};
      st2(ox + 16 * i * N_, u);
    }
    STAMP(13);
    STAMP(14);
  }
  STAMP_END();
}

extern "C" void kernel_launch(void* const* d_in, const int* in_sizes, int n_in, void* d_out, int out_size, void* d_ws,
                              size_t ws_size, hipStream_t stream) {
  (void)in_sizes; (void)n_in; (void)out_size; (void)d_ws; (void)ws_size;
  w4a16_kernel<<<256, 512, 0, stream>>>((const uint16_t*)d_in[0], (const int*)d_in[1], (const uint16_t*)d_in[2],
                                        (const uint16_t*)d_in[3], (uint16_t*)d_out
);
}
```

```cpp
#include <hip/hip_runtime.h>
#include <hip/hip_bf16.h>
#include <stdint.h>
#include <string.h>
#define KORD "012435867,102534678,201346578,013245768"

typedef float    f32x4  __attribute__((ext_vector_type(4)));
typedef float    f32x2  __attribute__((ext_vector_type(2)));
typedef __bf16   bf16x8 __attribute__((ext_vector_type(8)));
typedef __bf16   bf16x2 __attribute__((ext_vector_type(2)));
typedef unsigned u32x4  __attribute__((ext_vector_type(4)));
typedef unsigned u32x2  __attribute__((ext_vector_type(2)));
typedef int      i32x4  __attribute__((ext_vector_type(4)));
typedef short    s16x4  __attribute__((ext_vector_type(4)));
typedef short    s16x8  __attribute__((ext_vector_type(8)));

#define DI __device__ __forceinline__
#define LDS_AS __attribute__((address_space(3)))
#define GLB_AS __attribute__((address_space(1)))

constexpr int M_ = 2048, K_ = 576, N_ = 4608;
constexpr int NST = 9;
constexpr int A_STAGE = 32768;
constexpr int B_STAGE = 18432;
constexpr int A_OFF = 0, B_OFF = 3 * A_STAGE, SZ_OFF = B_OFF + 2 * B_STAGE, SZX_OFF = SZ_OFF + 18 * 1024;
constexpr int LDS_BYTES = SZX_OFF + 18 * 128;

DI unsigned rfl(unsigned x) { return __builtin_amdgcn_readfirstlane(x); }
DI unsigned cvtpk(float lo, float hi) {
  f32x2 v = {lo, hi};
  return __builtin_bit_cast(unsigned, __builtin_convertvector(v, bf16x2));
}
DI float bflo(unsigned u) { return __builtin_bit_cast(float, u << 16); }
DI float bfhi(unsigned u) { return __builtin_bit_cast(float, u & 0xffff0000u); }
DI float dq(int q, float s24, float nzs) {
  float r;
  asm("v_fma_mix_f32 %0, %1, %2, %3 op_sel_hi:[1,0,0]" : "=v"(r) : "v"(q), "v"(s24), "v"(nzs));
  return r;
}

template <int N> DI void wait_vm() { asm volatile("s_waitcnt vmcnt(%0)" ::"n"(N) : "memory"); }
DI void wait_lgkm0() { asm volatile("s_waitcnt lgkmcnt(0)" ::: "memory"); }
DI void barrier() { __builtin_amdgcn_s_barrier(); asm volatile("" ::: "memory"); }
#define GAP() __builtin_amdgcn_sched_barrier(0)
DI void opaque(unsigned& v) { asm volatile("" : "+v"(v)); }


DI void dma16(unsigned m0v, unsigned voff, __amdgpu_buffer_rsrc_t srd, unsigned soff) {
  asm volatile("s_mov_b32 m0, %0\n\ts_nop 0\n\tbuffer_load_dwordx4 %1, %2, %3 offen lds" ::"s"(m0v), "v"(voff), "s"(srd), "s"(soff) : "m0", "memory");
}
DI void ld4(i32x4& d, unsigned voff, __amdgpu_buffer_rsrc_t srd, unsigned soff) {
  asm volatile("buffer_load_dwordx4 %0, %1, %2, %3 offen" : "=v"(d) : "v"(voff), "s"(srd), "s"(soff) : "memory");
}
template <int OFF> DI void st_main(u32x2 v, unsigned voff, __amdgpu_buffer_rsrc_t srd, unsigned soff) {
  asm volatile("buffer_store_dwordx2 %0, %1, %2, %3 offen offset:%4 nt" ::"v"(v), "v"(voff), "s"(srd), "s"(soff), "n"(OFF) : "memory");
}
DI void st_extra(u32x2 v, unsigned voff, __amdgpu_buffer_rsrc_t srd, unsigned soff) {
  asm volatile("buffer_store_dwordx2 %0, %1, %2, %3 offen" ::"v"(v), "v"(voff), "s"(srd), "s"(soff) : "memory");
}

struct Grp { int a, s; };
constexpr Grp grp(int sc, int g) {
  return g == 16 ? Grp{1, 8} : g == 17 ? Grp{0, 8} : Grp{g & 1, g / 2};
}
constexpr int gbeg(int sc, int g) { int n = 0; for (int i = 0; i < g; ++i) n += grp(sc, i).a ? 8 : 9; return n; }
constexpr int NLD = 153;
constexpr int gof(int sc, int i) { int g = 0; while (gbeg(sc, g + 1) <= i) ++g; return g; }
constexpr int gend(int sc, int a, int s) { int g = 0; while (grp(sc, g).a != a || grp(sc, g).s != s) ++g; return gbeg(sc, g + 1); }
constexpr int imin(int a, int b) { return a < b ? a : b; }
#define PB0 17
#define PB1 30
#define L6 12
#define KM1 2
#define LEAD_E 8
#define K_E 2
#define LEAD_O 0
#define K_O 2
constexpr int PB(int sc, int p) {
  return p == -1 ? PB0 : p == 0 ? PB1 : imin(NLD, 34 + 17 * p);
}
struct Raw { i32x4 q[8]; i32x4 e; };
struct SZ { f32x4 s, z, sx, zx; };
struct Prod {
  __amdgpu_buffer_rsrc_t srdX, srdQ;
  unsigned long long ord;
  unsigned vB, vX, vA, cB, cX, cA0, cA1;
  unsigned ldsA, wB, wX, szm, szx;
  unsigned lane, w, nmain;
  Raw r[3];
  SZ z;
  float t[8];
  u32x4 f;
};
template <int ST> DI unsigned krot(const Prod& p) { return (unsigned)(p.ord >> (4 * ST)) & 15; }
template <int SC, int I, int P> DI void issue(Prod& p) {
  constexpr int G = gof(SC, I), ST = grp(SC, G).s, J = I - gbeg(SC, G);
  if constexpr (grp(SC, G).a) {
    static_assert(ST < 3 || P >= ST - 2, "x ring slot still in use");
    if constexpr (J == 0) { p.cA0 = p.vA + krot<ST>(p) * 128; p.cA1 = (p.vA ^ 64) + krot<ST>(p) * 128; }
    dma16(p.ldsA + (ST % 3) * A_STAGE + J * 1024, (J & 1) ? p.cA1 : p.cA0, p.srdX, (unsigned)(J * 8 * K_ * 2));
  } else {
    static_assert(ST < 3 || P >= ST - 3, "raw register set still in use");
    if constexpr (J == 0 && ST < 7) p.cB = p.vB + krot<ST>(p) * (64 * N_ * 4);
    if constexpr (J == 0 && ST == 7)
      p.cB = ((64 * ((unsigned)(p.ord >> (28 + 4 * (p.w >> 1))) & 15) + 32 * (p.w & 1) + 8 * (p.lane >> 4)) * N_ + p.nmain + 4 * (p.lane & 15)) * 4;
    if constexpr (J == 0 && ST == 8) p.cB += 256;
    if constexpr (J == 8) p.cX = p.vX + krot<ST>(p) * (64 * N_ * 4);
    if constexpr (J < 8) ld4(p.r[ST % 3].q[J], p.cB, p.srdQ, (unsigned)(J * N_ * 4));
    else                 ld4(p.r[ST % 3].e, p.cX, p.srdQ, 0u);
  }
  GAP();
}
template <int SC, int I, int E, int P> DI void issue_range(Prod& p) {
  if constexpr (I < E) { issue<SC, I, P>(p); issue_range<SC, I + 1, E, P>(p); }
}
DI void pin_raw(Raw& r) {
  asm volatile("" : "+v"(r.q[0]), "+v"(r.q[1]), "+v"(r.q[2]), "+v"(r.q[3]), "+v"(r.q[4]), "+v"(r.q[5]), "+v"(r.q[6]), "+v"(r.q[7]), "+v"(r.e));
}
template <int ST> DI void read_sz(Prod& p, LDS_AS char* lds) {
  if constexpr (ST < NST) {
    if constexpr (ST == 7) {
      p.szm = SZ_OFF + (14 + p.w) * 1024 + (p.lane & 15) * 16;
      p.wB = B_OFF + (p.w >> 1) * 9216 + (p.w & 1) * 4096 + p.lane * 16;
    }
    constexpr int O = ST < 7 ? ST * 2048 : (ST - 7) * 256;
    p.z.s  = *(LDS_AS f32x4*)(lds + p.szm + O);
    p.z.z  = *(LDS_AS f32x4*)(lds + p.szm + O + 512);
    p.z.sx = *(LDS_AS f32x4*)(lds + p.szx + ST * 256);
    p.z.zx = *(LDS_AS f32x4*)(lds + p.szx + ST * 256 + 64);
  }
}
constexpr int NSTEP = 19;
template <int D, int S> DI void step(Prod& p, LDS_AS char* lds) {
  const Raw& r = p.r[D % 3];
  if constexpr (S == 0) {
#pragma unroll
    for (int j = 0; j < 4; ++j) p.t[j] = dq(r.e[j], p.z.sx[j], p.z.zx[j]);
  } else if constexpr (S == 1) {
    *(LDS_AS u32x2*)(lds + p.wX + (D & 1) * B_STAGE) = u32x2{cvtpk(p.t[0], p.t[1]), cvtpk(p.t[2], p.t[3])};
  } else if constexpr (S < 18) {
    constexpr int C = (S - 2) >> 2, U = (S - 2) & 3;
    if constexpr (U < 2) {
#pragma unroll
      for (int j = 4 * U; j < 4 * U + 4; ++j) p.t[j] = dq(r.q[j][C], p.z.s[C], p.z.z[C]);
    } else if constexpr (U == 2) {
#pragma unroll
      for (int j = 0; j < 4; ++j) p.f[j] = cvtpk(p.t[2 * j], p.t[2 * j + 1]);
    } else {
      *(LDS_AS u32x4*)(lds + p.wB + (D & 1) * B_STAGE + C * 1024) = p.f;
    }
  } else read_sz<D + 1>(p, lds);
  GAP();
}
template <int SC, int P, int S, int I, int K> DI void weave(Prod& p, LDS_AS char* lds) {
  if constexpr (S < NSTEP) {
    step<P + 1, S>(p, lds);
    if constexpr ((S + 1) % K == 0 && I < PB(SC, P + 1)) { issue<SC, I, P>(p); weave<SC, P, S + 1, I + 1, K>(p, lds); }
    else weave<SC, P, S + 1, I, K>(p, lds);
  } else issue_range<SC, I, PB(SC, P + 1), P>(p);
}
template <int SC, int P, int LEAD, int K> DI void period(Prod& p, LDS_AS char* lds) {
  constexpr int I1 = imin(PB(SC, P) + LEAD, PB(SC, P + 1));
  constexpr int AE = gend(SC, 1, P == 6 ? 8 : P + 1);
  static_assert(gend(SC, 0, P + 1) <= I1 && AE <= PB(SC, P + 1), "operand not issued yet");
  issue_range<SC, PB(SC, P), I1, P>(p);
  wait_vm<I1 - gend(SC, 0, P + 1)>(); pin_raw(p.r[(P + 1) % 3]); GAP();
  weave<SC, P, 0, I1, K>(p, lds);
  wait_vm<PB(SC, P + 1) - AE>(); wait_lgkm0();
  barrier();
}
template <int SC, int LEAD, int K> DI void produce(Prod& p, LDS_AS char* lds) {
  period<SC, -1, 0, KM1>(p, lds);
  period<SC, 0, LEAD, K>(p, lds); period<SC, 1, LEAD, K>(p, lds); period<SC, 2, LEAD, K>(p, lds); period<SC, 3, LEAD, K>(p, lds);
  period<SC, 4, LEAD, K>(p, lds); period<SC, 5, LEAD, K>(p, lds); period<SC, 6, L6, K>(p, lds); period<SC, 7, 17, K>(p, lds);
}

struct Cons {
  f32x4 acc[4][9];
  bf16x8 a[2][4];
#define PD 3
  bf16x8 b[PD + 1];
  bf16x8 f[2][4][4], va[2][4][4], x[4];
  u32x2 o;
  unsigned rA[2][2], rB, rX, oM, oX;
  __amdgpu_buffer_rsrc_t srdO;
};
template <int KS, int I> DI bf16x8 ld_a(Cons& c, LDS_AS char* lds) {
  constexpr int SL = (KS >> 1) % 3;
  return *(LDS_AS bf16x8*)(lds + c.rA[SL >> 1][KS & 1] + (SL & 1) * A_STAGE + I * 2048);
}
template <int KS, int I> DI void rd_a(Cons& c, LDS_AS char* lds) { c.a[KS & 1][I] = ld_a<KS, I>(c, lds); }
template <int S> DI bf16x8 ld_b(Cons& c, LDS_AS char* lds) {
  constexpr int KS = S / 9, T = S % 9, OFF = ((KS >> 1) & 1) * B_STAGE + (KS & 1) * 9216 + T * 1024;
  if constexpr (T < 8) return *(LDS_AS bf16x8*)(lds + c.rB + OFF);
  else {
    s16x4 lo = __builtin_amdgcn_ds_read_tr16_b64_v4i16((LDS_AS s16x4*)(lds + c.rX + OFF));
    s16x4 hi = __builtin_amdgcn_ds_read_tr16_b64_v4i16((LDS_AS s16x4*)(lds + c.rX + OFF + 512));
    return __builtin_bit_cast(bf16x8, __builtin_shufflevector(lo, hi, 0, 1, 2, 3, 4, 5, 6, 7));
  }
}
template <int S> DI void rd_b(Cons& c, LDS_AS char* lds) { c.b[S % (PD + 1)] = ld_b<S>(c, lds); }
#define BM 48
#define LAG 3
#define RS -4
#define AD 8
template <int H, int KK, int C> DI void rd_f(Cons& c, LDS_AS char* lds) {
  c.f[H][KK][C] = *(LDS_AS bf16x8*)(lds + c.rB + (1 - H) * B_STAGE + (KK >> 1) * 9216 + ((KK & 1) * 4 + C) * 1024);
}
template <int M> DI void tail_rd(Cons& c, LDS_AS char* lds) {
  if constexpr (M == -12) c.va[0][0][0] = ld_a<14, 0>(c, lds);
  if constexpr (M == -11) rd_f<0, 0, 0>(c, lds);
  if constexpr (M >= -10 && M < -7) c.va[0][M + 11][0] = ld_a<14, M + 11>(c, lds);
  if constexpr (M >= -7 && M < -4) rd_f<0, 0, M + 8>(c, lds);
  if constexpr (M >= RS && M < RS + 15) {
    constexpr int KK = (M - RS) / 5 + 1, J = (M - RS) % 5;
    if constexpr (J == 0) c.va[0][0][KK] = ld_a<14 + KK, 0>(c, lds); else rd_f<0, KK, J - 1>(c, lds);
  }
  if constexpr (M >= BM && M < BM + 24) {
    constexpr int KK = (M - BM) / 6, J = (M - BM) % 6;
    if constexpr (J == 0) c.va[1][0][KK] = ld_a<14 + KK, 0>(c, lds);
    else if constexpr (J < 5) rd_f<1, KK, J - 1>(c, lds);
    else c.x[KK] = ld_b<9 * (14 + KK) + 8>(c, lds);
  }
  constexpr int N = M + AD;
  if constexpr (N >= 28 && N < 64 && N % 4 == 0) c.va[0][(N - 16) / 12][(N - 16) % 12 / 4 + 1] = ld_a<15 + (N - 16) % 12 / 4, (N - 16) / 12>(c, lds);
  if constexpr (N >= 84 && N < 144 && (N - 64) % 5 == 0) c.va[1][(N - 64) / 20][(N - 64) % 20 / 5] = ld_a<14 + (N - 64) % 20 / 5, (N - 64) / 20>(c, lds);
}
template <int H, int I, int J> DI void put(Cons& c) {
  constexpr int G = J / 2;
  if constexpr (J == 8) c.o = u32x2{cvtpk(c.acc[I][8][0], c.acc[I][8][1]), cvtpk(c.acc[I][8][2], c.acc[I][8][3])};
  else if constexpr (J == 9) st_extra(c.o, c.oX, c.srdO, (unsigned)(16 * I * N_ * 2));
  else if constexpr (J % 2 == 0) c.o = u32x2{cvtpk(c.acc[I][4 * H][G], c.acc[I][4 * H + 1][G]), cvtpk(c.acc[I][4 * H + 2][G], c.acc[I][4 * H + 3][G])};
  else st_main<128 * H>(c.o, c.oM, c.srdO, (unsigned)((16 * I + G) * N_ * 2));
}
template <int M> DI void last(Cons& c, LDS_AS char* lds) {
  static_assert(BM <= 56 && AD <= 8, "operand order");
  if constexpr (M == BM) { barrier(); GAP(); }
  if constexpr (M < 64) {
    constexpr int I = M < 16 ? M % 4 : (M - 16) / 12, KK = M < 16 ? 0 : (M - 16) % 12 / 4 + 1, C = M < 16 ? M / 4 : M % 4;
    c.acc[I][C] = __builtin_amdgcn_mfma_f32_16x16x32_bf16(c.va[0][I][KK], c.f[0][KK][C], c.acc[I][C], 0, 0, 0);
  } else if constexpr (M < 144) {
    constexpr int I = (M - 64) / 20, KK = (M - 64) % 20 / 5, C = (M - 64) % 5;
    if constexpr (C == 4) c.acc[I][8] = __builtin_amdgcn_mfma_f32_16x16x32_bf16(c.x[KK], c.va[1][I][KK], c.acc[I][8], 0, 0, 0);
    else c.acc[I][4 + C] = __builtin_amdgcn_mfma_f32_16x16x32_bf16(c.va[1][I][KK], c.f[1][KK][C], c.acc[I][4 + C], 0, 0, 0);
  }
  GAP();
  tail_rd<M>(c, lds);
  constexpr int J = M - LAG;
  if constexpr (J >= 28 && J < 76 && (J - 28) % 12 < 8) put<0, (J - 28) / 12, (J - 28) % 12>(c);
  if constexpr (J >= 84 && (J - 64) % 20 < 10) put<1, (J - 64) / 20 - 1, (J - 64) % 20>(c);
  GAP();
  if constexpr (J + 1 < 154) last<M + 1>(c, lds);
}
template <int S> DI void slot(Cons& c, LDS_AS char* lds) {
  constexpr int KS = S / 9, T = S % 9;
  static_assert(PD == 3, "last-stage prefetch assumes PD == 3");
  if constexpr (T == 9 - PD && (KS & 1) && KS + 1 < 18) {
    wait_lgkm0();
    barrier();
    GAP();
  }
#pragma unroll
  for (int i = 0; i < 4; ++i) {
    if constexpr (T < 8) c.acc[i][T] = __builtin_amdgcn_mfma_f32_16x16x32_bf16(c.a[KS & 1][i], c.b[S % (PD + 1)], c.acc[i][T], 0, 0, 0);
    else                 c.acc[i][T] = __builtin_amdgcn_mfma_f32_16x16x32_bf16(c.b[S % (PD + 1)], c.a[KS & 1][i], c.acc[i][T], 0, 0, 0);
    GAP();
    if constexpr (S + PD < 126) { if (i == 0) rd_b<S + PD>(c, lds); }
    else { if (i == 0) tail_rd<4 * S - 504>(c, lds); if (i == 1) tail_rd<4 * S - 503>(c, lds); if (i == 2) tail_rd<4 * S - 502>(c, lds); if (i == 3) tail_rd<4 * S - 501>(c, lds); }
    if constexpr (KS + 1 < 14) {
      if (i == 1) { if constexpr (T == 9 - PD) rd_a<KS + 1, 0>(c, lds); if constexpr (T == 10 - PD) rd_a<KS + 1, 2>(c, lds); }
      if (i == 2) { if constexpr (T == 9 - PD) rd_a<KS + 1, 1>(c, lds); if constexpr (T == 10 - PD) rd_a<KS + 1, 3>(c, lds); }
    }
    GAP();
  }
  if constexpr (S + 1 < 126) slot<S + 1>(c, lds);
  else last<0>(c, lds);
}

struct Order { unsigned long long m[32]; };
__global__ __launch_bounds__(512, 2) void w4a16_kernel(const uint16_t* x_, const int* qw_, const uint16_t* sc_, const uint16_t* zr_, uint16_t* out_, Order od) {
  __shared__ __attribute__((aligned(1024))) char smem[LDS_BYTES];
  LDS_AS char* lds = (LDS_AS char*)smem;
  const unsigned ldsb = rfl((unsigned)(uintptr_t)lds);
  const unsigned lane = threadIdx.x & 63, wave = rfl(threadIdx.x >> 6), w = wave & 3;
  const unsigned bid = blockIdx.x, sb = bid & 7, jn = (bid >> 3) & 3, mb = bid >> 5;
  const unsigned nmain = 576 * sb + 128 * jn, next = 576 * sb + 512 + 16 * jn, m0 = 256 * mb;
  unsigned long long ord = od.m[bid >> 3];
  const unsigned vB0 = (8 * (2 * w + (lane >> 5)) * N_ + nmain + 4 * (lane & 31)) * 4;
  asm volatile("" : "+s"(x_), "+s"(qw_), "+s"(sc_), "+s"(zr_), "+s"(out_), "+s"(ord) : "v"(vB0));
  u32x2 sv[3], zv[3];
  unsigned wa[3], wz[3];
  auto EARLY = [&]() {
#pragma unroll
    for (int i = 0; i < 3; ++i) {
      unsigned idx = (w * 3 + i) * 64 + lane;
      idx = idx < 647 ? idx : 647;
      const unsigned e = idx - 576;
      const bool mainp = idx < 576;
      const unsigned gl = mainp ? (idx >> 5) : (e >> 2);
      const unsigned gg = 2 * ((unsigned)(ord >> (2 * (gl & 30))) & 15) + (gl & 1);
      const unsigned off = (gg * N_ + (mainp ? nmain + 4 * (idx & 31) : next + 4 * (e & 3))) * 2;
      asm volatile("global_load_dwordx2 %0, %1, %2" : "=v"(sv[i]) : "v"(off), "s"(sc_) : "memory");
      asm volatile("global_load_dwordx2 %0, %1, %2" : "=v"(zv[i]) : "v"(off), "s"(zr_) : "memory");
      wa[i] = mainp ? SZ_OFF + gl * 1024 + (idx & 31) * 16 : SZX_OFF + gl * 128 + (e & 3) * 16;
      wz[i] = wa[i] + (mainp ? 512 : 64);
    }
  };
  unsigned wv = wave;
  if (wave < 4) EARLY();
  asm volatile("" : "+s"(wv));
  if (__builtin_expect(wv >= 4, 1)) {
    Prod p;
    const unsigned Q = lane & 31, o = 2 * w + (lane >> 5), ks = w >> 1, xr = lane >> 2, xc = lane & 3;
    p.ord = ord; p.lane = lane; p.w = w; p.nmain = nmain;
    p.vB = vB0;
    p.srdX = __builtin_amdgcn_make_buffer_rsrc((void*)x_, 0, M_ * K_ * 2, 0x00020000);
    p.srdQ = __builtin_amdgcn_make_buffer_rsrc((void*)qw_, 0, K_ * N_ * 4, 0x00020000);
    issue_range<2, 0, 1, -2>(p); issue_range<2, 1, 8, -2>(p);
    p.vX = ((16 * w + xr) * N_ + next + 4 * xc) * 4;
    p.vA = (m0 + 64 * w + (lane >> 3)) * (K_ * 2) + (((lane & 7) ^ (lane >> 4)) << 4);
    p.ldsA = ldsb + A_OFF + w * 8192;
    issue_range<2, 8, 9, -2>(p);
    issue_range<2, 9, PB0, -2>(p);
    p.wB = B_OFF + ks * 9216 + (Q >> 4) * 4096 + (((o & 3) * 16 + (Q & 15)) << 4);
    p.wX = B_OFF + ks * 9216 + 8192 + ((xr >> 2) & 1) * 512 + (2 * (w & 1) + (xr >> 3)) * 128 + (xr & 3) * 32 + xc * 8;
    p.szm = SZ_OFF + ks * 1024 + Q * 16; p.szx = SZX_OFF + ks * 128 + xc * 16;
    opaque(p.wB); opaque(p.wX); opaque(p.szm); opaque(p.szx);
    barrier();
    read_sz<0>(p, lds);
    if (w >> 1) { produce<2, LEAD_O, K_O>(p, lds); __builtin_amdgcn_endpgm(); }
    produce<2, LEAD_E, K_E>(p, lds);
  } else {
    __builtin_amdgcn_s_setprio(2);
    Cons c;
    const unsigned r = lane & 15, q = lane >> 4;
    c.rA[0][0] = A_OFF + (64 * w + r) * 128 + ((q ^ (r >> 1)) << 4);
    c.rA[0][1] = c.rA[0][0] ^ 64;
    c.rA[1][0] = c.rA[0][0] + 2 * A_STAGE;
    c.rA[1][1] = c.rA[0][1] + 2 * A_STAGE;
    c.rB = B_OFF + lane * 16;
    c.rX = B_OFF + lane * 8;
    c.oM = ((m0 + 64 * w + 4 * q) * N_ + nmain + 4 * r) * 2;
    c.oX = ((m0 + 64 * w + r) * N_ + next + 4 * q) * 2;
    c.srdO = __builtin_amdgcn_make_buffer_rsrc((void*)out_, 0, M_ * N_ * 2, 0x00020000);
    opaque(c.rA[0][0]); opaque(c.rA[0][1]); opaque(c.rA[1][0]); opaque(c.rA[1][1]); opaque(c.rB); opaque(c.rX);
#pragma unroll
    for (int i = 0; i < 4; ++i)
#pragma unroll
      for (int t = 0; t < 9; ++t) c.acc[i][t] = f32x4{0.f, 0.f, 0.f, 0.f};
    GAP();
    wait_vm<0>();
    asm volatile("" : "+v"(sv[0]), "+v"(sv[1]), "+v"(sv[2]), "+v"(zv[0]), "+v"(zv[1]), "+v"(zv[2]));
#pragma unroll
    for (int i = 0; i < 3; ++i) {
      const f32x4 s = {bflo(sv[i][0]), bfhi(sv[i][0]), bflo(sv[i][1]), bfhi(sv[i][1])};
      const f32x4 zz = {bflo(zv[i][0]), bfhi(zv[i][0]), bflo(zv[i][1]), bfhi(zv[i][1])};
      *(LDS_AS f32x4*)(lds + wa[i]) = s * 16777216.f;
      *(LDS_AS f32x4*)(lds + wz[i]) = -zz * s;
    }
    wait_lgkm0(); barrier();
    barrier();
    rd_a<0, 0>(c, lds); rd_b<0>(c, lds); rd_a<0, 1>(c, lds); rd_a<0, 2>(c, lds); rd_a<0, 3>(c, lds);
    rd_b<1>(c, lds); rd_b<2>(c, lds);
    GAP();
    slot<0>(c, lds);
  }
}

extern "C" void kernel_launch(void* const* d_in, const int* in_sizes, int n_in, void* d_out, int out_size, void* d_ws,
                              size_t ws_size, hipStream_t stream) {
  (void)in_sizes; (void)n_in; (void)out_size; (void)d_ws; (void)ws_size;
  const char* ko = KORD;
  const int ng = (strlen(ko) + 1) / 10;
  Order od;
  for (int i = 0; i < 32; ++i) {
    const char* o = ko + 10 * (((i >> 2) + (i & 3)) % ng);
    unsigned long long m = 0;
    for (int j = 0; j < NST; ++j) m |= (unsigned long long)(o[j] - '0') << (4 * j);
    od.m[i] = m;
  }
  w4a16_kernel<<<256, 512, 0, stream>>>((const uint16_t*)d_in[0], (const int*)d_in[1], (const uint16_t*)d_in[2],
                                        (const uint16_t*)d_in[3], (uint16_t*)d_out, od);
}
```
